```python
import torch

def _softmax(x, axis=-1):
    return torch.softmax(x, dim=axis)
def _gelu(x):
    return torch.nn.functional.gelu(x, approximate="tanh")
def _silu(x):
    return torch.nn.functional.silu(x)
def _sigmoid(x):
    return torch.sigmoid(x)
def _layernorm(x, axis=-1, eps=1e-5):
    m = x.mean(dim=axis, keepdim=True)
    v = x.var(dim=axis, keepdim=True, unbiased=False)
    return (x - m) / torch.sqrt(v + eps)
def _scatter_add(tgt, idx, src, axis=0):
    return tgt.clone().index_add_(axis, idx, src)
def _segment_sum(src, seg, n):
    out = torch.zeros((n,) + src.shape[1:], dtype=src.dtype)
    return out.index_add_(0, seg, src)
def _pad(x, pw):
    flat = tuple(v for pair in reversed(pw) for v in pair)
    return torch.nn.functional.pad(x, flat)

kernel_name = 'standalone__paged_block_table_kv_pagedattention__sampling'

def setup_inputs():

    _w = (lambda a: (lambda t: t.to(torch.bfloat16) if t.is_floating_point() else t)(torch.as_tensor(a)))
    B, Hq, Hkv, Sq, Skv, D = 32, 32, 8, 4, 2048, 128
    bs = 16
    nbps = max(1, Skv // bs)
    num_phys = B * nbps + 4
    bt = torch.randperm(num_phys)[: B * nbps].reshape(B, nbps).to(torch.int64)
    cl = (nbps * bs - (torch.arange(1, B + 1, dtype=torch.int64) * 2654435761) % max(1, nbps * bs // 2)).to(torch.int64)
    return {
        "q": _w((lambda *s: torch.randn(*s, dtype=torch.bfloat16) * 0.8)(B, Hq, Sq, D)),
        "k_cache": _w((lambda *s: torch.randn(*s, dtype=torch.bfloat16) * 0.8)(num_phys, bs, Hkv, D)),
        "v_cache": _w((lambda *s: torch.randn(*s, dtype=torch.bfloat16) * 0.05)(num_phys, bs, Hkv, D)),
        "block_table": _w(bt),
        "context_lens": _w(cl),
    }

def reference(q, k_cache, v_cache, block_table, context_lens):
    B, Hq, Sq, D = q.shape
    bs = k_cache.shape[1]
    Hkv = k_cache.shape[2]
    nbps = block_table.shape[1]
    Skv = nbps * bs
    bt = block_table.reshape(-1)
    k = (lambda x, *d: x.permute(*d))(k_cache[bt].reshape(B, Skv, Hkv, D), 0, 2, 1, 3)
    v = (lambda x, *d: x.permute(*d))(v_cache[bt].reshape(B, Skv, Hkv, D), 0, 2, 1, 3)
    g = Hq // Hkv
    qg = q.reshape(B, Hkv, g, Sq, D)
    scores = torch.einsum("bhgqd,bhkd->bhgqk", qg, k) * (D ** -0.5)
    pos = torch.arange(Skv)
    msk = pos[None, :] < context_lens[:, None]
    scores = torch.where(msk[:, None, None, None, :], scores, -1e9)
    p = _softmax(scores, axis=-1)
    out = torch.einsum("bhgqk,bhkd->bhgqd", p, v)
    return out.reshape(B, Hq, Sq, D)
```

```cpp
#include <hip/hip_runtime.h>
#include <hip/hip_bf16.h>
#include <stdint.h>

namespace {

constexpr int B_ = 32, HQ = 32, HKV = 8, SQ = 4, SKV = 2048, DH = 128, PAGE = 16;
constexpr int NBPS = SKV / PAGE;
constexpr int ROWS = (HQ / HKV) * SQ;
constexpr int TOK_BYTES = HKV * DH * 2;
constexpr int PAGE_BYTES = PAGE * TOK_BYTES;

#define NWAVES 8
#define DEPTH 2
#define KVAUX 1

typedef unsigned v4u __attribute__((ext_vector_type(4)));
typedef unsigned v2u __attribute__((ext_vector_type(2)));
typedef float f32x4 __attribute__((ext_vector_type(4)));
typedef __bf16 bf16x8 __attribute__((ext_vector_type(8)));

__device__ __forceinline__ f32x4 mfma16(v4u a, v4u b, f32x4 c) {
  return __builtin_amdgcn_mfma_f32_16x16x32_bf16(__builtin_bit_cast(bf16x8, a), __builtin_bit_cast(bf16x8, b), c, 0, 0, 0);
}
__device__ __forceinline__ unsigned cvtpk(float lo, float hi) {
  unsigned r;
  asm("v_cvt_pk_bf16_f32 %0, %1, %2" : "=v"(r) : "v"(lo), "v"(hi));
  return r;
}
__device__ __forceinline__ float xrow_max(float x) {
  auto s = __builtin_amdgcn_permlane16_swap(__float_as_uint(x), __float_as_uint(x), false, false);
  x = fmaxf(__uint_as_float(s[0]), __uint_as_float(s[1]));
  auto t = __builtin_amdgcn_permlane32_swap(__float_as_uint(x), __float_as_uint(x), false, false);
  return fmaxf(__uint_as_float(t[0]), __uint_as_float(t[1]));
}
__device__ __forceinline__ float xrow_sum(float x) {
  auto s = __builtin_amdgcn_permlane16_swap(__float_as_uint(x), __float_as_uint(x), false, false);
  x = __uint_as_float(s[0]) + __uint_as_float(s[1]);
  auto t = __builtin_amdgcn_permlane32_swap(__float_as_uint(x), __float_as_uint(x), false, false);
  return __uint_as_float(t[0]) + __uint_as_float(t[1]);
}

struct Tile { v4u k[8]; v4u v[8]; };

constexpr int OSTR = DH + 4;

__global__ void __launch_bounds__(NWAVES * 64, NWAVES / 4)
attn_kernel(const unsigned short* __restrict__ q, const unsigned short* __restrict__ kc, const unsigned short* __restrict__ vc,
            const int* __restrict__ bt, const int* __restrict__ cl, unsigned short* __restrict__ out) {
  __shared__ __attribute__((aligned(16))) float lds[NWAVES * ROWS * OSTR + NWAVES * ROWS * 2];
  const int tid = threadIdx.x, lane = tid & 63;
  const int w = __builtin_amdgcn_readfirstlane(tid >> 6);
  const int r = lane >> 4, c = lane & 15;
  const int h = blockIdx.x & 7, b = blockIdx.x >> 3;

  int btv;
  {
    int pg = 2 * (w + NWAVES * (lane >> 1)) + (lane & 1);
    pg = pg < NBPS ? pg : NBPS - 1;
    btv = bt[b * NBPS + pg];
  }
  int len = cl[b];
  int lim = len;
  if (len <= 0) len = SKV;
  if (len > SKV) len = SKV;
  const int ntile = (len + 31) >> 5;
  const int n = (ntile - w + NWAVES - 1) / NWAVES;

  v4u qf[4];
  {
    const unsigned short* qp = q + ((size_t)(b * HQ + h * (HQ / HKV)) * SQ + c) * DH + r * 8;
#pragma unroll
    for (int kk = 0; kk < 4; ++kk) qf[kk] = *reinterpret_cast<const v4u*>(qp + kk * 32);
  }
  const int koff = c * TOK_BYTES + r * 16;
  const int voff0 = (4 * r) * TOK_BYTES + c * 16;
  const int voff1 = voff0 + 2 * TOK_BYTES;
  const char* kbase = reinterpret_cast<const char*>(kc) + h * (DH * 2);
  const char* vbase = reinterpret_cast<const char*>(vc) + h * (DH * 2);

  auto issue = [&](Tile& t, int i) {
#pragma unroll
    for (int p = 0; p < 2; ++p) {
      const int lp = 2 * (w + NWAVES * i) + p;
      int nv = len - lp * PAGE;
      nv = __builtin_amdgcn_readfirstlane(nv < 0 ? 0 : (nv > PAGE ? PAGE : nv));
      const int j = (2 * i + p) & 63;
      const unsigned phys = (unsigned)__builtin_amdgcn_readlane(btv, j);
      const size_t po = (size_t)phys * PAGE_BYTES;
      const __amdgpu_buffer_rsrc_t rk = __builtin_amdgcn_make_buffer_rsrc((void*)(kbase + po), 0, nv * TOK_BYTES, 0x00020000);
      const __amdgpu_buffer_rsrc_t rv = __builtin_amdgcn_make_buffer_rsrc((void*)(vbase + po), 0, nv * TOK_BYTES, 0x00020000);
#pragma unroll
      for (int kk = 0; kk < 4; ++kk) t.k[p * 4 + kk] = __builtin_amdgcn_raw_buffer_load_b128(rk, koff + kk * 64, 0, KVAUX);
      t.v[p * 4 + 0] = __builtin_amdgcn_raw_buffer_load_b128(rv, voff0, 0, KVAUX);
      t.v[p * 4 + 1] = __builtin_amdgcn_raw_buffer_load_b128(rv, voff0 + TOK_BYTES, 0, KVAUX);
      t.v[p * 4 + 2] = __builtin_amdgcn_raw_buffer_load_b128(rv, voff1, 0, KVAUX);
      t.v[p * 4 + 3] = __builtin_amdgcn_raw_buffer_load_b128(rv, voff1 + TOK_BYTES, 0, KVAUX);
    }
    __builtin_amdgcn_sched_barrier(0);
  };

  constexpr float CS = 0.08838834764831845f * 1.4426950408889634f;
  constexpr float MASKED = -17179869184.0f;
  float m = -1e30f, l = 0.f;
  f32x4 o[8];
#pragma unroll
  for (int e = 0; e < 8; ++e) o[e] = f32x4{0.f, 0.f, 0.f, 0.f};

  auto consume = [&](Tile& t, int i) {
    f32x4 sa = {0.f, 0.f, 0.f, 0.f}, sb = {0.f, 0.f, 0.f, 0.f};
#pragma unroll
    for (int kk = 0; kk < 4; ++kk) { sa = mfma16(t.k[kk], qf[kk], sa); sb = mfma16(t.k[4 + kk], qf[kk], sb); }
    const int tok0 = (w + NWAVES * i) * 32;
    if (tok0 + 32 > lim) {
      const int rel = lim - tok0 - 4 * r;
#pragma unroll
      for (int j = 0; j < 4; ++j) { sa[j] = j < rel ? sa[j] : MASKED; sb[j] = j + 16 < rel ? sb[j] : MASKED; }
    }
    float mt = fmaxf(fmaxf(fmaxf(sa[0], sa[1]), fmaxf(sa[2], sa[3])), fmaxf(fmaxf(sb[0], sb[1]), fmaxf(sb[2], sb[3])));
    mt = xrow_max(mt) * CS;
    const float mn = fmaxf(m, mt);
    const float alpha = __builtin_amdgcn_exp2f(m - mn);
    m = mn;
    float p[8];
#pragma unroll
    for (int j = 0; j < 4; ++j) { p[j] = __builtin_amdgcn_exp2f(fmaf(sa[j], CS, -mn)); p[4 + j] = __builtin_amdgcn_exp2f(fmaf(sb[j], CS, -mn)); }
    l = l * alpha + (((p[0] + p[1]) + (p[2] + p[3])) + ((p[4] + p[5]) + (p[6] + p[7])));
    const v4u pf = {cvtpk(p[0], p[1]), cvtpk(p[2], p[3]), cvtpk(p[4], p[5]), cvtpk(p[6], p[7])};
#pragma unroll
    for (int e = 0; e < 8; ++e) {
      const int wd = e >> 1;
      const unsigned sel = (e & 1) ? 0x07060302u : 0x05040100u;
      const v4u vf = {__builtin_amdgcn_perm(t.v[1][wd], t.v[0][wd], sel), __builtin_amdgcn_perm(t.v[3][wd], t.v[2][wd], sel),
                      __builtin_amdgcn_perm(t.v[5][wd], t.v[4][wd], sel), __builtin_amdgcn_perm(t.v[7][wd], t.v[6][wd], sel)};
      o[e] = mfma16(vf, pf, o[e] * alpha);
    }
    __builtin_amdgcn_sched_barrier(0);
  };

  Tile ring[DEPTH];
#pragma unroll
  for (int s = 0; s < DEPTH; ++s) issue(ring[s], s);
  int i = 0;
  do {
#pragma unroll
    for (int s = 0; s < DEPTH; ++s) {
      if (i + s < n) consume(ring[s], i + s);
      issue(ring[s], i + s + DEPTH);
    }
    i += DEPTH;
  } while (i < n);

  l = xrow_sum(l);
  float* ow = lds + (w * ROWS + c) * OSTR + 32 * r;
#pragma unroll
  for (int j = 0; j < 4; ++j) {
    *reinterpret_cast<f32x4*>(ow + 8 * j) = f32x4{o[0][j], o[1][j], o[2][j], o[3][j]};
    *reinterpret_cast<f32x4*>(ow + 8 * j + 4) = f32x4{o[4][j], o[5][j], o[6][j], o[7][j]};
  }
  float* ml = lds + NWAVES * ROWS * OSTR;
  if (r == 0) { ml[(w * ROWS + c) * 2] = m; ml[(w * ROWS + c) * 2 + 1] = l; }
  __syncthreads();
  for (int idx = tid; idx < ROWS * DH / 4; idx += NWAVES * 64) {
    const int row = idx >> 5, d = (idx & 31) * 4;
    float M = -1e30f;
#pragma unroll
    for (int ww = 0; ww < NWAVES; ++ww) M = fmaxf(M, ml[(ww * ROWS + row) * 2]);
    float L = 0.f;
    f32x4 acc = {0.f, 0.f, 0.f, 0.f};
#pragma unroll
    for (int ww = 0; ww < NWAVES; ++ww) {
      const float wt = __builtin_amdgcn_exp2f(ml[(ww * ROWS + row) * 2] - M);
      L += wt * ml[(ww * ROWS + row) * 2 + 1];
      acc += wt * *reinterpret_cast<const f32x4*>(lds + (ww * ROWS + row) * OSTR + d);
    }
    const float inv = 1.0f / L;
    const v2u res = {cvtpk(acc[0] * inv, acc[1] * inv), cvtpk(acc[2] * inv, acc[3] * inv)};
    *reinterpret_cast<v2u*>(out + ((size_t)(b * HQ + h * (HQ / HKV)) * SQ + row) * DH + d) = res;
  }
}

}

extern "C" void kernel_launch(void* const* d_in, const int* in_sizes, int n_in, void* d_out, int out_size, void* d_ws,
                              size_t ws_size, hipStream_t stream) {
  (void)in_sizes; (void)n_in; (void)out_size; (void)d_ws; (void)ws_size;
  attn_kernel<<<B_ * HKV, NWAVES * 64, 0, stream>>>((const unsigned short*)d_in[0], (const unsigned short*)d_in[1],
                                                    (const unsigned short*)d_in[2], (const int*)d_in[3], (const int*)d_in[4],
                                                    (unsigned short*)d_out);
}
```

```cpp
#include <hip/hip_runtime.h>
#include <hip/hip_bf16.h>
#include <stdint.h>

namespace {

constexpr int B_ = 32, HQ = 32, HKV = 8, SQ = 4, SKV = 2048, DH = 128, PAGE = 16;
constexpr int NBPS = SKV / PAGE;
constexpr int TOK_BYTES = HKV * DH * 2;
constexpr int PAGE_BYTES = PAGE * TOK_BYTES;

constexpr int NW = 8;
constexpr int MINX = 4;
constexpr int PACE = 272;
constexpr int TOFF = 0;
constexpr int THETA = 26;
constexpr int PW = -1000;
constexpr int PR = -500;
constexpr int PD = 64;
constexpr int NB1 = 2;
constexpr int PRO = 2 * NW;
constexpr int SPIN_CAP = 1 << 20;

constexpr int PSZ = 8192;
constexpr int L_HP = 0;
constexpr int L_PS = L_HP + NW * PSZ;
constexpr int QSZ = PSZ;
constexpr int L_QS = L_PS + (NW + 1) * QSZ;
constexpr int L_AL = L_QS + 2 * 4096;
constexpr int L_ML = L_AL + NW * 1024;
constexpr int L_W = L_ML + 4 * 512;
constexpr int L_SO = L_W + 640;
constexpr int L_SV = L_SO + 1280;
constexpr int L_HI = L_SV + 640;
constexpr int L_SY = L_HI + 384;
constexpr int L_END = L_SY + 128;

constexpr int WS_P = 4096;
constexpr int GSZ = 9 * 1024;
constexpr unsigned STEP = 0x9E3779B9u;

typedef unsigned v4u __attribute__((ext_vector_type(4)));
typedef unsigned v2u __attribute__((ext_vector_type(2)));
typedef float f32x4 __attribute__((ext_vector_type(4)));
typedef float f32x2 __attribute__((ext_vector_type(2)));
typedef __bf16 bf16x8 __attribute__((ext_vector_type(8)));
typedef __bf16 bf16x2 __attribute__((ext_vector_type(2)));
typedef short s16x4 __attribute__((ext_vector_type(4)));
typedef __attribute__((address_space(1))) unsigned gu32;
typedef __attribute__((address_space(3))) char lchar;
typedef __attribute__((address_space(3))) int lint;
typedef __attribute__((address_space(3))) float lfloat;
typedef __attribute__((address_space(3))) v4u lv4u;
typedef __attribute__((address_space(3))) f32x4 lf32x4;
typedef __attribute__((address_space(3))) f32x2 lf32x2;
typedef __attribute__((address_space(3))) v2u lv2u;
typedef __attribute__((address_space(3))) unsigned lu32;
typedef __attribute__((address_space(3))) void lvoid;

__device__ __forceinline__ f32x4 mfma32(v4u a, v4u b, f32x4 c) {
  return __builtin_amdgcn_mfma_f32_16x16x32_bf16(__builtin_bit_cast(bf16x8, a), __builtin_bit_cast(bf16x8, b), c, 0, 0, 0);
}
__device__ __forceinline__ f32x4 mfma16(v2u a, v2u b, f32x4 c) {
  return __builtin_amdgcn_mfma_f32_16x16x16bf16_1k(__builtin_bit_cast(s16x4, a), __builtin_bit_cast(s16x4, b), c, 0, 0, 0);
}
__device__ __forceinline__ v2u lds_tr(lchar* p) {
  return __builtin_bit_cast(v2u, __builtin_amdgcn_ds_read_tr16_b64_v4i16((__attribute__((address_space(3))) s16x4*)p));
}
__device__ __forceinline__ unsigned cvtpk(float lo, float hi) {
  return __builtin_bit_cast(unsigned, __builtin_convertvector(f32x2{lo, hi}, bf16x2));
}
__device__ __forceinline__ float xrow_max(float x) {
  auto s = __builtin_amdgcn_permlane16_swap(__float_as_uint(x), __float_as_uint(x), false, false);
  x = fmaxf(__uint_as_float(s[0]), __uint_as_float(s[1]));
  auto t = __builtin_amdgcn_permlane32_swap(__float_as_uint(x), __float_as_uint(x), false, false);
  return fmaxf(__uint_as_float(t[0]), __uint_as_float(t[1]));
}
__device__ __forceinline__ float xrow_sum(float x) {
  auto s = __builtin_amdgcn_permlane16_swap(__float_as_uint(x), __float_as_uint(x), false, false);
  x = __uint_as_float(s[0]) + __uint_as_float(s[1]);
  auto t = __builtin_amdgcn_permlane32_swap(__float_as_uint(x), __float_as_uint(x), false, false);
  return __uint_as_float(t[0]) + __uint_as_float(t[1]);
}
__device__ __forceinline__ int scan32(int x) {
  x += __builtin_amdgcn_update_dpp(0, x, 0x111, 0xf, 0xf, false);
  x += __builtin_amdgcn_update_dpp(0, x, 0x112, 0xf, 0xf, false);
  x += __builtin_amdgcn_update_dpp(0, x, 0x114, 0xf, 0xf, false);
  x += __builtin_amdgcn_update_dpp(0, x, 0x118, 0xf, 0xf, false);
  x += __builtin_amdgcn_update_dpp(0, x, 0x142, 0xa, 0xf, false);
  return x;
}
template <int N> __device__ __forceinline__ unsigned count_less(unsigned other, unsigned key) {
  if constexpr (N == 0) return other < key;
  else return ((unsigned)__builtin_amdgcn_update_dpp(0, (int)other, 0x120 + N, 0xf, 0xf, false) < key) + count_less<N - 1>(other, key);
}
__device__ __forceinline__ int rl(int v, int l) { return __builtin_amdgcn_readlane(v, l); }
__device__ __forceinline__ int imin(int a, int b) { return a < b ? a : b; }
__device__ __forceinline__ int imax(int a, int b) { return a > b ? a : b; }
__device__ __forceinline__ int lds_ld(lint* p) { return __hip_atomic_load(p, __ATOMIC_RELAXED, __HIP_MEMORY_SCOPE_WORKGROUP); }
__device__ __forceinline__ void lds_st(lint* p, int v) { __hip_atomic_store(p, v, __ATOMIC_RELAXED, __HIP_MEMORY_SCOPE_WORKGROUP); }
__device__ __forceinline__ void lds_fence() {
  asm volatile("" ::: "memory");
  __builtin_amdgcn_s_waitcnt(0xc07f);
  asm volatile("" ::: "memory");
}
template <int SLEEP> __device__ __forceinline__ void lds_wait_ge(lint* p, int v) {
  for (int it = 0; __builtin_amdgcn_readfirstlane(lds_ld(p)) < v && it < SPIN_CAP; ++it) __builtin_amdgcn_s_sleep(SLEEP);
  asm volatile("" ::: "memory");
}
__device__ __forceinline__ int eff_len(int len) { return (len <= 0 || len > SKV) ? SKV : len; }

__device__ __forceinline__ int below(unsigned long long m) {
  return __builtin_amdgcn_mbcnt_hi((unsigned)(m >> 32), __builtin_amdgcn_mbcnt_lo((unsigned)m, 0));
}

constexpr float CS = 0.08838834764831845f * 1.4426950408889634f;
constexpr float MASKED = -17179869184.0f;
constexpr float MINIT = -1e30f;

__global__ void __launch_bounds__((NW + 1) * 64, NW / 4 + 1)
attn_kernel(const unsigned short* __restrict__ q, const unsigned short* __restrict__ kc, const unsigned short* __restrict__ vc,
            const int* __restrict__ bt, const int* __restrict__ cl, long long sc64, char* __restrict__ ws,
            unsigned short* __restrict__ out) {
  __shared__ __attribute__((aligned(16))) char smem[L_END];
  lchar* const lds = (lchar*)smem;
  lint* const sy = (lint*)(lds + L_SY);
  unsigned T0 = 0;
  const int tid = threadIdx.x, lane = tid & 63;
  const int w = __builtin_amdgcn_readfirstlane(tid >> 6);
  const int r = lane >> 4, c = lane & 15;
  const int h = blockIdx.x & 7, g = blockIdx.x >> 3;

  unsigned warm0;
  {
    unsigned long long pc;
    int len;
    asm volatile("s_getpc_b64 %0\n.Lattn_pc:\n\ts_mov_b32 %1, .Lattn_last-.Lattn_pc\n\ts_max_i32 %1, %1, .Lattn_end-.Lattn_pc" : "=s"(pc), "=s"(len) :: "scc");
    warm0 = *(const gu32*)(pc + (unsigned)imin(tid * 128, len));
  }
  lint* const hist = (lint*)(lds + L_HI);
  *(lv2u*)(hist + 2 * lane) = v2u{0u, 0u};
  int tix = (w & 13) || g > 9 ? -1 : g * 7, o1 = g * 4, o2 = (g * NBPS + w) * 4;
  asm volatile("" : "+s"(tix), "+s"(o1), "+s"(o2));
  __builtin_amdgcn_sched_barrier(0);
  asm volatile("s_waitcnt lgkmcnt(0)" :: "s"(q), "s"(kc), "s"(vc), "s"(bt), "s"(cl), "s"(out), "s"(ws), "s"(sc64) : "memory");
  typedef const __attribute__((address_space(4))) unsigned cu32;
  const int leng = *(cu32*)((const char*)cl + (unsigned)o1);
  const int phs[2] = {(int)*(cu32*)((const char*)bt + (unsigned)o2), (int)*(cu32*)((const char*)bt + (unsigned)o2 + 32)};
  int lenv;
  __builtin_amdgcn_sched_barrier(0);
  const unsigned st64 = (unsigned)(sc64 >> 32);
  const unsigned swarm = *(cu32*)((const char*)(tix < 0 ? (const void*)cl : w & 2 ? vc : kc) + imax(tix, 0) * st64);
  __builtin_amdgcn_sched_barrier(0);
  int pg, npg, npp, mv = 0, oc = 0, total;
  auto schedule = [&]() {
    const int np = (eff_len(lenv) + PAGE - 1) >> 4;
    const unsigned key = (np << 5) | (lane & 31);
    auto s = __builtin_amdgcn_permlane16_swap(key, key, false, false);
    const unsigned cnt = count_less<15>(lane < 32 ? s[0] : s[1], key);
    auto t = __builtin_amdgcn_permlane32_swap(cnt, cnt, false, false);
    const int rank = t[0] + t[1];
    pg = __builtin_ctz((unsigned)__builtin_amdgcn_ballot_w64(rank == B_ - 1 - rl(rank, g)));
    total = rl(scan32(np), 31);
    npg = rl(np, g);
    npp = rl(np, pg);
    int x = imin(imax(npp - npg, npg - npp) >> 1, imax(npp, npg) - PRO);
    x = x < MINX ? 0 : x;
    mv = npp > npg ? x : -x;
    oc = npg + imin(mv, 0);
  };
  auto wait_until = [&](unsigned t) {
    while ((unsigned)__builtin_amdgcn_s_memrealtime() - T0 < t) __builtin_amdgcn_s_sleep(1);
  };
  auto put = [&](int off, const f32x4 (&o)[8], f32x4 f) {
    const int x = off + r * 2048 + (((2 * c) ^ ((c >> 2) & 1) ^ (4 * r)) << 4);
#pragma unroll
    for (int j = 0; j < 4; ++j) {
#pragma unroll
      for (int k = 0; k < 2; ++k)
        *(lf32x4*)(lds + (x ^ ((j ^ k) << 4)) + j * 512) = f32x4{o[4 * k][j] * f[j], o[4 * k + 1][j] * f[j], o[4 * k + 2][j] * f[j], o[4 * k + 3][j] * f[j]};
    }
    asm volatile("" ::: "memory");
  };
  auto slice = [&](int e) { return c * 512 + (((4 * e + r) ^ ((e >> 1) & 1) ^ c) << 4); };
  int ta, sa, va, oa;
  __amdgpu_buffer_rsrc_t ro;
  auto prep = [&]() {
    const int wt = w & (NW - 1);
    ta = L_PS + NW * QSZ + wt * 1024 + lane * 16;
    sa = L_W + 512 + c * 8;
    va = L_HP + (lane >> 2) * 256 + (((2 * wt + ((c >> 1) & 1)) ^ ((lane >> 1) & 14)) << 4) + (c & 1) * 8;
    oa = c * (2 * DH) + wt * 32 + r * 8;
    asm volatile("" : "+v"(ta), "+v"(sa), "+v"(va), "+v"(oa));
    ro = __builtin_amdgcn_make_buffer_rsrc((void*)(out + (size_t)(g * HQ + h * (HQ / HKV)) * SQ * DH), 0, w < NW ? 16 * DH * 2 : 0, 0x00020000);
    asm volatile("" : "+s"(ro));
  };
  auto load_q = [&](int seq, int off) {
    const __amdgpu_buffer_rsrc_t rq = __builtin_amdgcn_make_buffer_rsrc((void*)q, 0, B_ * HQ * SQ * DH * 2, 0x00020000);
#pragma unroll
    for (int kk = 0; kk < 4; ++kk)
      __builtin_amdgcn_raw_ptr_buffer_load_lds(rq, (lvoid*)(lds + off + kk * 1024), 16, c * 256 + r * 16, (seq * HKV + h) * 4096 + kk * 64, 0, 2);
  };
  auto vm_fence = [&]() {
    asm volatile("" ::: "memory");
    __builtin_amdgcn_s_waitcnt(0x0f70);
    asm volatile("" ::: "memory");
  };
  unsigned tag = 0;

  f32x4 acc;
  v4u pl[NW / 2];
  __builtin_amdgcn_sched_barrier(0);
  if (w == NW) {
    __builtin_amdgcn_s_setprio(3);
    lenv = cl[lane & 31];
    const __amdgpu_buffer_rsrc_t rb = __builtin_amdgcn_make_buffer_rsrc((void*)bt, 0, B_ * NBPS * 4, 0x00020000);
#pragma unroll
    for (int u = 0; u < 4; ++u) {
      lvoid* const dst = (lvoid*)(lds + L_PS + u * 4096);
      __builtin_amdgcn_raw_ptr_buffer_load_lds(rb, dst, 16, lane * 16, u * 4096, 0, 0);
      __builtin_amdgcn_raw_ptr_buffer_load_lds(rb, dst, 16, lane * 16, u * 4096, 1024, 0);
      __builtin_amdgcn_raw_ptr_buffer_load_lds(rb, dst, 16, lane * 16, u * 4096, 2048, 0);
      __builtin_amdgcn_raw_ptr_buffer_load_lds(rb, dst, 16, lane * 16, u * 4096, 3072, 0);
    }
    load_q(g, L_QS);
    __builtin_amdgcn_sched_barrier(0);
  }
  __builtin_amdgcn_s_barrier();
  int live = NW - w;
  asm volatile("" : "+s"(live));
  if (__builtin_expect(live, 1)) {
    int krd[4], kwr[4];
#pragma unroll
    for (int i = 0; i < 4; ++i) {
      kwr[i] = L_HP + w * PSZ + (4 * r + i) * 256 + ((c ^ (4 * r + i)) * 16);
      krd[i] = L_HP + w * PSZ + c * 256 + (((4 * i + r) ^ c) * 16);
    }
    const int voff0 = (4 * r) * TOK_BYTES + c * 16;
    const int voff1 = voff0 + 2 * TOK_BYTES;
    const char* kbase = reinterpret_cast<const char*>(kc) + h * (DH * 2);
    const char* vbase = reinterpret_cast<const char*>(vc) + h * (DH * 2);

    auto issue = [&](v4u (&t)[4], const char* base, int phys, int nv) {
      const __amdgpu_buffer_rsrc_t rs =
          __builtin_amdgcn_make_buffer_rsrc((void*)(base + (size_t)(unsigned)phys * PAGE_BYTES), 0, nv * TOK_BYTES, 0x00020000);
      t[0] = __builtin_amdgcn_raw_buffer_load_b128(rs, voff0, 0, 2);
      t[1] = __builtin_amdgcn_raw_buffer_load_b128(rs, voff0 + TOK_BYTES, 0, 2);
      t[2] = __builtin_amdgcn_raw_buffer_load_b128(rs, voff1, 0, 2);
      t[3] = __builtin_amdgcn_raw_buffer_load_b128(rs, voff1 + TOK_BYTES, 0, 2);
      __builtin_amdgcn_sched_barrier(0);
    };

    v4u ring[2][4];
    __builtin_amdgcn_sched_barrier(0);
#pragma unroll
    for (int s = 0; s < 2; ++s) {
      const int nv = __builtin_amdgcn_readfirstlane(imax(imin(eff_len(leng) - (w + NW * s) * PAGE, PAGE), 0));
      const int ph = phs[s];
      issue(ring[s], kbase, nv ? ph : 0, nv);
      if (s == 0) { T0 = (unsigned)__builtin_amdgcn_s_memrealtime(); wait_until(PD); }
    }
    asm volatile("" ::: "memory");
    lds_wait_ge<1>(sy + 27, 1);
    asm volatile("" :: "v"(warm0), "s"(swarm));
    const v4u hd = *(lv4u*)(sy + 20);
    const int T = __builtin_amdgcn_readfirstlane(hd[0]);
    T0 = __builtin_amdgcn_readfirstlane(hd[3]);
    const int n = (T - w + NW - 1) / NW, n2 = (n + 1) & ~1, ve = n2 + imax(n2, 4), v0 = ve - n;
    const bool isv = lane >= n2;
    const int pi = isv ? lane - v0 : lane, j = pi * NW + w;
    int la = isv ? L_SV + (j & 127) * 4 : L_SO + (j & 127) * 8;
    int due = (unsigned)pi < (unsigned)n ? imin(imax((int)fmaf((float)(isv ? j + T : j), __uint_as_float(hd[1]), TOFF * 0.1f), 1), 0x7fff) << 16 : 0;
    auto describe = [&](unsigned key) {
      const int len = key & 128 ? (int)hd[2] : leng, first = (key & 127) * PAGE;
      const int nv = due ? imax(imin(eff_len(len) - first, PAGE), 0) : 0;
      const int lm = imax(imin(imax(len, 0) - first, PAGE), 0);
      return nv | (lm << 5) | (nv ? (key & 128) << 3 : 2048) | due;
    };
    int physv = 0, metav = describe(w);
    asm volatile("" : "+v"(la), "+v"(due), "+v"(metav));
    lds_wait_ge<1>(sy + 27, 2);
    auto paced = [&](v4u (&t)[4], int idx) {
      const int mnext = rl(metav, idx);
      if (mnext & 31) wait_until(mnext >> 16);
      issue(t, idx < n2 ? kbase : vbase, rl(physv, idx) & 0xffffff, mnext & 31);
    };

    float mo = MINIT, lo = 0.f, mh = MINIT, lh = 0.f;
    int i = 0;
    do {
#pragma unroll
      for (int s = 0; s < 2; ++s) {
        const int meta = rl(metav, i + s);
        v2u pk;
        float mn;
        if (meta & 31) {
          const bool hp = meta & 1024;
          if (hp) lds_wait_ge<1>(sy + 16, 1);
          v4u qf[4];
#pragma unroll
          for (int kk = 0; kk < 4; ++kk) qf[kk] = *(lv4u*)(lds + L_QS + (meta & 1024) * 4 + kk * 1024 + lane * 16);
#pragma unroll
          for (int kk = 0; kk < 4; ++kk) *(lv4u*)(lds + kwr[kk]) = ring[s][kk];
          f32x4 sc = {0.f, 0.f, 0.f, 0.f};
#pragma unroll
          for (int kk = 0; kk < 4; ++kk) sc = mfma32(*(lv4u*)(lds + krd[kk]), qf[kk], sc);
          const int lm = (meta >> 5) & 31;
          if (lm < PAGE) {
            const int rel = lm - 4 * r;
#pragma unroll
            for (int j = 0; j < 4; ++j) sc[j] = j < rel ? sc[j] : MASKED;
          }
          const float m = hp ? mh : mo;
          mn = fmaxf(m, xrow_max(fmaxf(fmaxf(sc[0], sc[1]), fmaxf(sc[2], sc[3]))) * CS);
          const float alpha = __builtin_amdgcn_exp2f(m - mn);
          float p[4];
#pragma unroll
          for (int j = 0; j < 4; ++j) p[j] = __builtin_amdgcn_exp2f(fmaf(sc[j], CS, -mn));
          const float l = (hp ? lh : lo) * alpha + ((p[0] + p[1]) + (p[2] + p[3]));
          mh = hp ? mn : mh; mo = hp ? mo : mn;
          lh = hp ? l : lh; lo = hp ? lo : l;
          pk = v2u{cvtpk(p[0], p[1]), cvtpk(p[2], p[3])};
          __builtin_amdgcn_sched_barrier(0);
        }
        if (i + s == 0) {
          lds_wait_ge<1>(sy + 17, 1);
          const unsigned key = *(lu32*)(lds + la), vr = *(lu32*)(lds + la + 4);
          metav = describe(key);
          physv = metav & 31 ? (key >> 8) | (vr << 24) : 0;
        }
        paced(ring[s], i + s + 2);
        if (meta & 31) {
          const int vr = (unsigned)rl(physv, i + s) >> 24, at = (vr & (NW - 1)) * 16 + vr / NW;
          *(lv2u*)(lds + L_PS + at * 512 + lane * 8) = pk;
          asm volatile("" ::: "memory");
          *(lfloat*)(lds + L_AL + at * 64 + c * 4) = mn;
          __builtin_amdgcn_sched_barrier(0);
        }
      }
      i += 2;
    } while (i < n2);
    const unsigned long long hm = __builtin_amdgcn_ballot_w64((metav & 1024) && lane >= n2);
    const int lasth = hm ? 63 - __builtin_clzll(hm) : -1;
    {
      lo = xrow_sum(lo);
      lh = xrow_sum(lh);
      *(lfloat*)(lds + L_ML + r * 512 + w * 64 + c * 4) = r < 2 ? (r ? lo : mo) : (r == 2 ? mh : lh);
      lds_fence();
      lds_st(sy + w, lasth >= 0 ? 2 + lasth - v0 : 1);
    }

    prep();
    f32x4 oo[8], oh[8];
#pragma unroll
    for (int e = 0; e < 8; ++e) oo[e] = oh[e] = f32x4{0.f, 0.f, 0.f, 0.f};
    auto getp = [&](int k, float d) {
      const v2u p = *(lv2u*)(lds + L_PS + w * QSZ + k * 512 + lane * 8);
      const float f = __builtin_amdgcn_exp2f(d);
      return v2u{cvtpk(__uint_as_float(p[0] << 16) * f, __uint_as_float(p[0] & 0xffff0000u) * f),
                 cvtpk(__uint_as_float(p[1] << 16) * f, __uint_as_float(p[1] & 0xffff0000u) * f)};
    };
    auto rows = [&](float d) {
      const int f = __float_as_int(__builtin_amdgcn_exp2f(d));
      f32x4 x;
#pragma unroll
      for (int j = 0; j < 4; ++j) x[j] = __int_as_float(__builtin_amdgcn_ds_bpermute(r * 16 + j * 4, f));
      return x;
    };
    auto fin = [&](int k) {
      lds_wait_ge<1>(sy + 19, 1);
      return *(lfloat*)(lds + L_W + k * 64 + c * 4);
    };
    lint* const ma = (lint*)(lds + L_AL + w * 1024 + c * 4);
    auto pv = [&](f32x4 (&o)[8], const v4u (&t)[4], v2u pf, f32x4 al) {
#pragma unroll
      for (int e = 0; e < 8; ++e) o[e] *= al;
      __builtin_amdgcn_sched_barrier(0);
#pragma unroll
      for (int e = 0; e < 8; ++e) {
        const int wd = e >> 1;
        const unsigned sel = (e & 1) ? 0x07060302u : 0x05040100u;
        const v2u vf = {__builtin_amdgcn_perm(t[1][wd], t[0][wd], sel), __builtin_amdgcn_perm(t[3][wd], t[2][wd], sel)};
        o[e] = mfma16(pf, vf, o[e]);
      }
      __builtin_amdgcn_sched_barrier(0);
    };
    auto vstep = [&](int s) {
      const int meta = rl(metav, i + s);
      int kind = meta & (1024 | 2048);
      int mi = lds_ld(ma + (i + s - v0) * 16);
      for (int it = 0; kind < 2048 && __builtin_amdgcn_ballot_w64(mi == -1) && it < SPIN_CAP; ++it) mi = lds_ld(ma + (i + s - v0) * 16);
      asm volatile("" ::: "memory");
      const float m = kind ? mh : mo, mn = fmaxf(m, __int_as_float(mi));
      const v2u pf = getp(i + s - v0, __int_as_float(mi) - mn);
      const f32x4 al = rows(m - mn);
      if (kind == 1024) {
        mh = mn;
        pv(oh, ring[s], pf, al);
      }
      asm volatile("" : "+s"(kind));
      if (kind == 0) {
        mo = mn;
        pv(oo, ring[s], pf, al);
      }
      if (i + s == lasth) {
        put(L_HP + w * PSZ, oh, rows(mh - fin(1)));
        lds_st(sy + 8 + w, 1);
      }
    };
    const int kind[2] = {rl(metav, ve - 2) & (1024 | 2048), rl(metav, ve - 1) & (1024 | 2048)};
    v2u pf[2];
    const int wr = (w - T) & (NW - 1);
#pragma unroll 1
    do {
      vstep(0);
      paced(ring[0], i + 2);
      vstep(1);
      i += 2;
      if (i == ve - 2) {
        const float Mo = fin(0), Mp = *(lfloat*)(lds + L_W + 64 + c * 4);
        const f32x4 f = rows(mo - Mo);
        mh -= Mp;
#pragma unroll
        for (int s = 0; s < 2; ++s) {
          pf[s] = getp(n - 2 + s, __int_as_float(ma[(n - 2 + s) * 16]) - (kind[s] ? Mp : Mo));
          *(lv2u*)(lds + L_QS + s * 4096 + (wr >> 1) * 1024 + lane * 16 + (wr & 1) * 8) = kind[s] ? v2u{0u, 0u} : pf[s];
        }
        asm volatile("" : "+v"(pf[0]), "+v"(pf[1]) :: "memory");
        put(L_PS + w * QSZ, oo, f);
        asm volatile("s_mov_b64 exec, 1\n\tds_add_u32 %0, %1\n\ts_mov_b64 exec, -1" :: "v"(L_SY + 100), "v"(1) : "memory");
      }
      paced(ring[1], i + 1);
    } while (i < ve - 2);
    {
      if (lasth >= ve - 2) {
#pragma unroll
        for (int s = 0; s < 2; ++s) {
          if (kind[s] == 1024) {
            pv(oh, ring[s], pf[s], rows(mh));
            mh = 0.f;
#pragma unroll
            for (int kk = 0; kk < 4; ++kk) ring[s][kk] = v4u{0u, 0u, 0u, 0u};
          }
        }
        put(L_HP + w * PSZ, oh, f32x4{1.f, 1.f, 1.f, 1.f});
        lds_st(sy + 8 + w, 1);
      }
      lds_wait_ge<1>(sy + 24, 1);
      int vw[4];
#pragma unroll
      for (int u = 0; u < 4; ++u) vw[u] = L_HP + wr * PSZ + r * 1024 + ((c ^ ((8 * r + 2 * u) & 14)) << 4);
#pragma unroll
      for (int u = 0; u < 4; ++u) *(lv4u*)(lds + vw[u] + u * 256) = ring[0][u];
      asm volatile("s_mov_b64 exec, 1\n\tds_add_u32 %0, %1\n\ts_mov_b64 exec, -1" :: "v"(L_SY + 104), "v"(1) : "memory");
      lds_wait_ge<1>(sy + 25, NW);
      f32x4 z[NW];
      v4u pa4[NW / 2];
#pragma unroll
      for (int ww = 0; ww < NW; ++ww) z[ww] = *(lf32x4*)(lds + L_PS + slice(w) + ww * QSZ);
#pragma unroll
      for (int u = 0; u < NW / 2; ++u) {
        pa4[u] = *(lv4u*)(lds + L_QS + u * 1024 + lane * 16);
        pl[u] = *(lv4u*)(lds + L_QS + 4096 + u * 1024 + lane * 16);
      }
      acc = ((z[0] + z[1]) + (z[2] + z[3])) + ((z[4] + z[5]) + (z[6] + z[7]));
      asm volatile("" : "+v"(vw[0]), "+v"(vw[1]), "+v"(vw[2]), "+v"(vw[3]), "+v"(acc), "+v"(pl[0]), "+v"(pl[1]), "+v"(pl[2]), "+v"(pl[3]));
      if (wr < 2 * NB1) {
#pragma unroll
        for (int u = 0; u < 4; ++u) *(lv4u*)(lds + vw[u] + 4096 + u * 256) = ring[1][u];
        asm volatile("s_mov_b64 exec, 1\n\tds_add_u32 %0, %1\n\ts_mov_b64 exec, -1" :: "v"(L_SY + 104), "v"(1) : "memory");
      }
      lds_wait_ge<1>(sy + 26, NW + 2 * NB1);
      v2u vf[NW + 2 * NB1];
#pragma unroll
      for (int p = 0; p < NW + 2 * NB1; ++p) vf[p] = lds_tr(lds + va + (p / NW) * 4096 + (p % NW) * PSZ);
      __builtin_amdgcn_sched_barrier(0);
#pragma unroll
      for (int u = 0; u < NW / 2 + NB1; ++u)
        acc = mfma32(v4u{vf[2 * u][0], vf[2 * u][1], vf[2 * u + 1][0], vf[2 * u + 1][1]}, u < NW / 2 ? pa4[u] : pl[u - NW / 2], acc);
      asm volatile("" : "+v"(acc));
      __builtin_amdgcn_sched_barrier(0);
      if (wr >= 2 * NB1) {
#pragma unroll
        for (int u = 0; u < 4; ++u) *(lv4u*)(lds + vw[u] + 4096 + u * 256) = ring[1][u];
      }
    }
    vm_fence();
    lds_fence();
    __builtin_amdgcn_s_barrier();
  } else {
    {
      asm volatile("" :: "v"(lenv), "s"(leng), "s"(phs[0]), "s"(phs[1]), "s"(swarm));
      T0 = (unsigned)__builtin_amdgcn_s_memrealtime();
    }
    __builtin_amdgcn_sched_barrier(0);
    schedule();
    {
      const int T = imax(oc, PRO) + imax(mv, 0);
      const float tk = (float)(total * PACE) * (0.05f / B_) * __builtin_amdgcn_rcpf((float)T);
      *(lv4u*)(sy + 20) = v4u{(unsigned)T, __float_as_uint(tk), (unsigned)rl(lenv, pg), T0};
      asm volatile("" ::: "memory");
      lds_st(sy + 27, 1);
      asm volatile("" ::: "memory");
      int ia = L_AL + lane * 16;
      asm volatile("" : "+v"(ia));
#pragma unroll
      for (int u = 0; u < NW; ++u) *(lv4u*)(lds + ia + u * 1024) = v4u{~0u, ~0u, ~0u, ~0u};
    }
    vm_fence();
    asm volatile("" :: "v"(warm0));
    lds_st(sy + 27, 2);
    lu32* tab = (lu32*)(lds + L_PS);
    const int gl = mv < 0 ? g : pg, npl = imax(npg, npp), x = imax(mv, -mv);
    const int gs = mv < 0 ? pg : g, nps = mv < 0 ? npp : npg;
    unsigned key[4] = {tab[gs * NBPS + lane], tab[gs * NBPS + 64 + lane], tab[gl * NBPS + lane], tab[gl * NBPS + 64 + lane]};
    int cls[4], pos[4];
    bool pro[4];
#pragma unroll
    for (int u = 0; u < 4; ++u) {
      cls[u] = imin((key[u] * ((unsigned)sc64 & 0xffff)) >> 16, 63);
      key[u] = (key[u] << 8) | (lane + (u & 1) * 64) | (u > 1 && mv > 0 ? 128 : 0);
    }
    const int own = mv < 0 ? 0 : npg;
    const bool c0 = lane >= PRO && lane < npl, c1 = lane + 64 < npl;
    int theta = 24;
    {
      const int s0 = lane < nps ? cls[0] : -1, s1 = lane + 64 < nps ? cls[1] : -1;
#pragma unroll
      for (int b = 16; b > 1; b >>= 1) {
        const int cnt = __builtin_popcountll(__builtin_amdgcn_ballot_w64(s0 >= theta + b)) + __builtin_popcountll(__builtin_amdgcn_ballot_w64(s1 >= theta + b));
        theta += cnt >= THETA ? b : 0;
      }
      theta = imax(theta, (int)((float)(x * 80) * __builtin_amdgcn_rcpf((float)imax(npl - PRO, 1))) + 1);
    }
    const bool y0 = c0 && cls[2] < theta, y1 = c1 && cls[3] < theta;
    const unsigned long long B0 = __builtin_amdgcn_ballot_w64(y0), B1 = __builtin_amdgcn_ballot_w64(y1);
    const unsigned long long C0 = __builtin_amdgcn_ballot_w64(c0 && !y0), C1 = __builtin_amdgcn_ballot_w64(c1 && !y1);
    const int nl = __builtin_popcountll(B0) + __builtin_popcountll(B1);
    const int p0 = y0 ? below(B0) : nl + below(C0);
    const int p1 = y1 ? __builtin_popcountll(B0) + below(B1) : nl + __builtin_popcountll(C0) + below(C1);
    const bool sel[4] = {lane >= PRO && lane < own, lane + 64 < own, c0 && (p0 >= x) == (mv < 0), c1 && (p1 >= x) == (mv < 0)};
#pragma unroll
    for (int u = 0; u < 4; ++u) {
      pro[u] = u == (mv < 0 ? 2 : 0) && lane < PRO;
      cls[u] = sel[u] || pro[u] ? cls[u] : 64 + (lane & 31);
      pos[u] = __hip_atomic_fetch_add(hist + cls[u], pro[u] ? 1 : 0x10001, __ATOMIC_RELAXED, __HIP_MEMORY_SCOPE_WORKGROUP);
    }
    const int cnt = lds_ld(hist + lane);
    int base = scan32(cnt);
    base += __builtin_amdgcn_update_dpp(0, base, 0x143, 0xc, 0xf, false);
    base += (PRO << 16) - cnt;
#pragma unroll
    for (int u = 0; u < 4; ++u) {
      const int rank = __builtin_amdgcn_ds_bpermute(cls[u] * 4, base) + pos[u];
      const int vr = sel[u] || pro[u] ? rank & 0xffff : 128 + (lane & 31);
      *(lv2u*)(lds + L_SO + (sel[u] ? rank >> 16 : pro[u] ? lane : vr) * 8) = v2u{key[u], (unsigned)vr};
      *(lu32*)(lds + L_SV + vr * 4) = key[u];
    }
    lds_fence();
    lds_st(sy + 17, 1);
    __builtin_amdgcn_sched_barrier(0);
    if (mv > 0) load_q(pg, L_QS + 4096);
    tag = __hip_atomic_load((gu32*)ws + h * B_ + (mv > 0 ? g : pg), __ATOMIC_RELAXED, __HIP_MEMORY_SCOPE_AGENT);
    prep();
    asm volatile(".Lattn_last:");
  }

  asm volatile("" : "+s"(live) :: "memory");
  {
    v2u vf[NW];
#pragma unroll
    for (int p = 2 * NB1; p < NW; ++p) vf[p] = lds_tr(lds + va + 4096 + p * PSZ);
    v4u tt = *(lv4u*)(lds + ta);
    const f32x2 sc = *(lf32x2*)(lds + sa);
    __builtin_amdgcn_sched_barrier(0);
#pragma unroll
    for (int u = NB1; u < NW / 2; ++u) acc = mfma32(v4u{vf[2 * u][0], vf[2 * u][1], vf[2 * u + 1][0], vf[2 * u + 1][1]}, pl[u], acc);
    asm volatile("" : "+v"(tt));
    const f32x4 th = {__uint_as_float(tt[0] << 16), __uint_as_float(tt[0] & 0xffff0000u), __uint_as_float(tt[2] << 16), __uint_as_float(tt[2] & 0xffff0000u)};
    acc = acc * sc[0] + th * sc[1];
    __builtin_amdgcn_raw_buffer_store_b64(v2u{cvtpk(acc[0], acc[1]), cvtpk(acc[2], acc[3])}, ro, oa, 0, 0);
  }
  if (!live) {
    if (mv > 0) {
      vm_fence();
      lds_st(sy + 16, 1);
    }
    tag += STEP;
    const int tend = (int)((float)(total * PACE) * (0.1f / B_)) + TOFF / 10;
    const int sid = h * B_ + (mv > 0 ? g : pg);
    auto rp = [&](int on) { return __builtin_amdgcn_make_buffer_rsrc((void*)(ws + WS_P + sid * GSZ), 0, on ? GSZ : 0, 0x00020000); };
    unsigned long long hw[2] = {0, 0};
#pragma unroll 1
    for (int ww = 0; ww < NW; ++ww) {
      lds_wait_ge<4>(sy + ww, 1);
      const int j = (__builtin_amdgcn_readfirstlane(lds_ld(sy + ww)) - 2) * NW + ww;
      hw[0] |= j >= 0 && j < 64 ? 1ull << j : 0;
      hw[1] |= j >= 64 ? 1ull << (j - 64) : 0;
    }
    auto ml = [&](int k, int ww) { return *(lfloat*)(lds + L_ML + k * 512 + ww * 64 + c * 4); };
    float Mo = ml(0, 0), Lo = 0.f, Mp = ml(2, 0), Lp = 0.f;
#pragma unroll
    for (int ww = 1; ww < NW; ++ww) {
      Mo = fmaxf(Mo, ml(0, ww));
      Mp = fmaxf(Mp, ml(2, ww));
    }
    *(lfloat*)(lds + L_W + (r & 1) * 64 + c * 4) = r & 1 ? Mp : Mo;
    asm volatile("" ::: "memory");
    lds_st(sy + 19, 1);
#pragma unroll
    for (int ww = 0; ww < NW; ++ww) {
      Lo += __builtin_amdgcn_exp2f(ml(0, ww) - Mo) * ml(1, ww);
      Lp += __builtin_amdgcn_exp2f(ml(2, ww) - Mp) * ml(3, ww);
    }
    if (mv <= 0) lds_st(sy + 24, 1);

#pragma unroll 1
    for (int it = 0; it < SPIN_CAP; ++it) {
      asm volatile("" ::: "memory");
      v4u t[9];
#pragma unroll
      for (int e = 0; e < 9; ++e) t[e] = __builtin_amdgcn_raw_buffer_load_b128(rp(it), e ? e * 1024 + lane * 16 : c * 16, 0, 16);
      __builtin_amdgcn_sched_barrier(0);
      const float Mh = it ? __uint_as_float(t[0][0]) : MINIT, M = fmaxf(Mo, Mh);
      const float a = __builtin_amdgcn_exp2f(Mo - M), b = __builtin_amdgcn_exp2f(Mh - M);
      const float inv = __builtin_amdgcn_rcpf(Lo * a + __uint_as_float(t[0][2]) * b);
      *(lf32x2*)(lds + sa) = f32x2{a * inv, b * inv};
      unsigned bad = (t[0][1] ^ tag) | (t[0][3] ^ tag);
      __builtin_amdgcn_sched_barrier(0);
#pragma unroll
      for (int e = 0; e < 8; ++e) {
        *(lv4u*)(lds + ta + e * 1024) = t[e + 1];
        bad |= (t[e + 1][1] ^ tag) | (t[e + 1][3] ^ tag);
        asm volatile("" : "+v"(bad));
        __builtin_amdgcn_sched_barrier(0);
      }
      if (__builtin_amdgcn_ballot_w64((it ? bad : mv >> 31) != 0) == 0) break;
      wait_until(imax(tend + PR / 10, 0));
    }
    if (mv > 0) {
      f32x4 v[8];
#pragma unroll
      for (int e = 0; e < 8; ++e) v[e] = f32x4{0.f, 0.f, 0.f, 0.f};
#pragma unroll 1
      while (hw[0] | hw[1]) {
        const int ww = __builtin_ctzll(hw[0] ? hw[0] : hw[1]) & (NW - 1);
        if (!hw[0]) hw[1] &= hw[1] - 1;
        hw[0] &= hw[0] - 1;
        lds_wait_ge<1>(sy + 8 + ww, 1);
        f32x4 t[8];
#pragma unroll
        for (int e = 0; e < 8; ++e) t[e] = *(lf32x4*)(lds + L_HP + ww * PSZ + slice(e));
        __builtin_amdgcn_sched_barrier(0);
#pragma unroll
        for (int e = 0; e < 8; ++e) v[e] += t[e];
      }
      lds_fence();
      lds_st(sy + 24, 1);
      v4u pk[9];
      pk[0] = v4u{__float_as_uint(Mp), tag, __float_as_uint(Lp), tag};
#pragma unroll
      for (int e = 0; e < 8; ++e) pk[e + 1] = v4u{cvtpk(v[e][0], v[e][1]), tag, cvtpk(v[e][2], v[e][3]), tag};
#pragma unroll
      for (int e = 0; e < 9; ++e) asm volatile("" : "+v"(pk[e]));
      wait_until(imax(tend + PW / 10, 0));
#pragma unroll
      for (int e = 0; e < 9; ++e) __builtin_amdgcn_raw_buffer_store_b128(pk[e], rp(1), e ? e * 1024 + lane * 16 : r ? GSZ : c * 16, 0, 16);
    }
    asm volatile(".Lattn_end:");
    lds_fence();
    __builtin_amdgcn_s_barrier();
    if (mv < 0 && lane == 0) __hip_atomic_store((gu32*)ws + sid, tag, __ATOMIC_RELAXED, __HIP_MEMORY_SCOPE_AGENT);
  }

}

}

extern "C" void kernel_launch(void* const* d_in, const int* in_sizes, int n_in, void* d_out, int out_size, void* d_ws,
                              size_t ws_size, hipStream_t stream) {
  (void)n_in; (void)out_size; (void)ws_size;
  long long sc64 = (64 << 16) / (in_sizes[1] / (PAGE * HKV * DH)) + ((long long)(in_sizes[1] / 32 & ~127) << 32);
  attn_kernel<<<B_ * HKV, (NW + 1) * 64, 0, stream>>>((const unsigned short*)d_in[0], (const unsigned short*)d_in[1],
                                                      (const unsigned short*)d_in[2], (const int*)d_in[3], (const int*)d_in[4],
                                                      sc64, (char*)d_ws, (unsigned short*)d_out);
}
```
